```python
import torch

def _softmax(x, axis=-1):
    return torch.softmax(x, dim=axis)

def _gelu(x):
    return torch.nn.functional.gelu(x, approximate='tanh')

def _silu(x):
    return torch.nn.functional.silu(x)

def _sigmoid(x):
    return torch.sigmoid(x)

def _layernorm(x, axis=-1, eps=1e-05):
    m = x.mean(dim=axis, keepdim=True)
    v = x.var(dim=axis, keepdim=True, unbiased=False)
    return (x - m) / torch.sqrt(v + eps)

def _scatter_add(tgt, idx, src, axis=0):
    return tgt.clone().index_add_(axis, idx, src)

def _segment_sum(src, seg, n):
    out = torch.zeros((n,) + src.shape[1:], dtype=src.dtype)
    return out.index_add_(0, seg, src)

def _pad(x, pw):
    flat = tuple((v for pair in reversed(pw) for v in pair))
    return torch.nn.functional.pad(x, flat)
kernel_name = 'adaLN_cond__sliding_block_cached__dense_activation_gelu__decode'

def setup_inputs():

    _w = lambda a: (lambda t: t.to(torch.bfloat16) if t.is_floating_point() else t)(torch.as_tensor(a))

    def _A_setup():
        B, Sq, D = (4, 1, 640)
        Hq, Hkv, Sk, Dh = (32, 32, 8192, 128)
        assert Hq % Hkv == 0, (Hq, Hkv)
        return {'x': _w((lambda *s: torch.randn(*s, dtype=torch.bfloat16) * 0.1)(B, Sq, D)), 'k_cache': _w((lambda *s: torch.randn(*s, dtype=torch.bfloat16) * 0.8)(B, Hkv, Sk, Dh)), 'v_cache': _w((lambda *s: torch.randn(*s, dtype=torch.bfloat16) * 3.2)(B, Hkv, Sk, Dh)), 'wq': _w((lambda *s: torch.randn(*s, dtype=torch.bfloat16) * 0.8)(D, Hq * Dh) * D ** (-0.5)), 'wo': _w((lambda *s: torch.randn(*s, dtype=torch.bfloat16) * 3.2)(Hq * Dh, D) * (Hq * Dh) ** (-0.5))}
    _A_in = _A_setup()

    def _B_setup():
        B, S, D, F = (4, 1, 640, 1728)
        return {'x': _w((lambda *s: torch.randn(*s, dtype=torch.bfloat16) * 0.1)(B, S, D)), 'w1': _w((lambda *s: torch.randn(*s, dtype=torch.bfloat16) * 3.2)(D, F) * D ** (-0.5)), 'b1': _w((lambda *s: torch.randn(*s, dtype=torch.bfloat16) * 3.2)(F) * 0.01), 'w2': _w((lambda *s: torch.randn(*s, dtype=torch.bfloat16) * 3.2)(F, D) * F ** (-0.5)), 'b2': _w((lambda *s: torch.randn(*s, dtype=torch.bfloat16) * 3.2)(D) * 0.01)}
    _B_in = _B_setup()
    return {'A_x': _A_in['x'], 'A_k_cache': _A_in['k_cache'], 'A_v_cache': _A_in['v_cache'], 'A_wq': _A_in['wq'], 'A_wo': _A_in['wo'], 'B_x': _B_in['x'], 'B_w1': _B_in['w1'], 'B_b1': _B_in['b1'], 'B_w2': _B_in['w2'], 'B_b2': _B_in['b2'], 'c': _w((lambda *s: torch.randn(*s, dtype=torch.bfloat16) * 0.1)(4, 640) * 8.0), 'w_mod': _w((lambda *s: torch.randn(*s, dtype=torch.bfloat16) * 0.1)(640, 6 * 640) * 640 ** (-0.5) * 8.0)}

def reference(A_x, A_k_cache, A_v_cache, A_wq, A_wo, B_x, B_w1, B_b1, B_w2, B_b2, c, w_mod):

    def _A_ref(x, k_cache, v_cache, wq, wo):
        B, Sq, D = x.shape
        Hq, Dh = (32, 128)
        Hkv, Sk = (k_cache.shape[1], k_cache.shape[2])
        W = min(4096, max(256, Sk // 4))
        rep = Hq // Hkv
        q = (lambda x, *d: x.permute(*d))((x @ wq).reshape(B, Sq, Hq, Dh), 0, 2, 1, 3)
        k = (lambda x, n, axis: torch.repeat_interleave(x, n, dim=axis))(k_cache[:, :, Sk - W:, :], rep, axis=1)
        v = (lambda x, n, axis: torch.repeat_interleave(x, n, dim=axis))(v_cache[:, :, Sk - W:, :], rep, axis=1)
        scores = torch.einsum('bhqd,bhkd->bhqk', q, k) * Dh ** (-0.5)
        p = _softmax(scores, axis=-1)
        o = torch.einsum('bhqk,bhkd->bhqd', p, v)
        return (lambda x, *d: x.permute(*d))(o, 0, 2, 1, 3).reshape(B, Sq, Hq * Dh) @ wo

    def _B_ref(x, w1, b1, w2, b2):
        h = torch.einsum('bsd,df->bsf', x, w1) + b1
        h = _gelu(h)
        return torch.einsum('bsf,fd->bsd', h, w2) + b2
    x = A_x
    mod = (_silu(c) @ w_mod).reshape(c.shape[0], 6, 1, -1)
    sa, ca, ga, sb, cb, gb = (mod[:, 0].clone(), mod[:, 1].clone(), mod[:, 2].clone(), mod[:, 3].clone(), mod[:, 4].clone(), mod[:, 5].clone())
    h = _layernorm(x, axis=-1) * (1 + ca) + sa
    x = x + ga * _A_ref(h, A_k_cache, A_v_cache, A_wq, A_wo)
    h = _layernorm(x, axis=-1) * (1 + cb) + sb
    x = x + gb * _B_ref(h, B_w1, B_b1, B_w2, B_b2)
    return x
```

```cpp
#include <hip/hip_runtime.h>
#include <hip/hip_bf16.h>
#include <stdint.h>
#include <type_traits>

using bf16 = __hip_bfloat16;
using u32 = unsigned;
using u64 = unsigned long long;
using u32x2 = __attribute__((ext_vector_type(2))) unsigned;
using u32x4 = __attribute__((ext_vector_type(4))) unsigned;
using f32x4 = __attribute__((ext_vector_type(4))) float;
using bf16x2 = __attribute__((ext_vector_type(2))) __bf16;
typedef __attribute__((ext_vector_type(2))) float f32x2;
typedef short s16x8 __attribute__((ext_vector_type(8)));
typedef short s16x4 __attribute__((ext_vector_type(4)));
typedef __attribute__((address_space(3))) s16x4* lds_s4p;
typedef __attribute__((ext_vector_type(8))) __bf16 bf16x8v;
typedef __attribute__((ext_vector_type(16))) __bf16 bf16x16v;
typedef short s16x16 __attribute__((ext_vector_type(16)));

constexpr int NB = 4, DM = 640, NH = 32, DH = 128, SK = 8192, WIN = 2048, FF = 1728, QD = 4096, MODN = 3840;
constexpr int NT = 512, NWG = 256;

constexpr u32 WS_EPOCH = 4;
constexpr u32 G_Q = 256;
constexpr u32 G_MOD = G_Q + 16 * 1024 * 16;
constexpr u32 G_MF = G_MOD + 8 * 160 * 16;
constexpr u32 G_ML = G_MF + 16 * 160 * 16;
constexpr u32 G_ACC = G_ML + 256 * 64;
constexpr u32 G_ACF = G_ACC + 2560 * 8;
constexpr int R1 = 16, R2 = 32;
constexpr u32 G_P2 = G_ACF + 2560 * 4 + 2048;
constexpr u32 G_P1 = G_P2 + R2 * 512;
constexpr u32 G_OUT = G_P1 + R1 * 512;
constexpr u32 G_OX = G_OUT + 40 * 56 * 256;
constexpr u32 G_NT = G_OX + 54 * 1024;
constexpr u32 WS_END = G_NT + 160 * 128;
constexpr u32 OOB = 0xF0000000u;

#define WSTAMP(k) do {} while (0)
#define WT3
#define WF3
#define STAMPT(i, tt) do {} while (0)
#define STAMP_DECL
#define STAMP_FLUSH do {} while (0)
#define BAR() asm volatile("s_waitcnt lgkmcnt(0)\n\ts_barrier" ::: "memory")
#define VM0() asm volatile("s_waitcnt vmcnt(0)" ::: "memory")

__device__ __forceinline__ float bfr(float x) { return __bfloat162float(__float2bfloat16(x)); }
__device__ __forceinline__ float bf2f(bf16 x) { return __bfloat162float(x); }
__device__ __forceinline__ float lo16(unsigned u) { return __builtin_bit_cast(float, u << 16); }
__device__ __forceinline__ float hi16(unsigned u) { return __builtin_bit_cast(float, u & 0xffff0000u); }
__device__ __forceinline__ u32 f2u(float f) { return __builtin_bit_cast(u32, f); }
__device__ __forceinline__ float u2f(u32 f) { return __builtin_bit_cast(float, f); }
__device__ __forceinline__ u32 cvt2(float a, float b) {
  const f32x2 v = {a, b};
  return __builtin_bit_cast(u32, __builtin_convertvector(v, bf16x2));
}
__device__ __forceinline__ float frcp(float x) { return __builtin_amdgcn_rcpf(x); }
__device__ __forceinline__ float fsqrt(float x) { return __builtin_amdgcn_sqrtf(x); }
__device__ __forceinline__ float fexp(float x) { return __builtin_amdgcn_exp2f(x * 1.4426950408889634f); }
__device__ __forceinline__ float dot2(unsigned k, unsigned q, float acc) {
  return __builtin_amdgcn_fdot2_f32_bf16(__builtin_bit_cast(bf16x2, k), __builtin_bit_cast(bf16x2, q), acc, false);
}
__device__ __forceinline__ float addlo(unsigned w, float acc) { u32 k = 0x00003f80u; asm("" : "+s"(k)); return dot2(w, k, acc); }
__device__ __forceinline__ float addhi(unsigned w, float acc) { return dot2(w, 0x3f800000u, acc); }
template <int CTRL> __device__ __forceinline__ float dpp(float x) {
  return __builtin_bit_cast(float, __builtin_amdgcn_mov_dpp(__builtin_bit_cast(int, x), CTRL, 0xf, 0xf, true));
}
template <int CTRL> __device__ __forceinline__ u32 dppu(u32 x) { return (u32)__builtin_amdgcn_mov_dpp((int)x, CTRL, 0xf, 0xf, true); }
constexpr int XOR1 = 0xB1, XOR2 = 0x4E, XOR7 = 0x141, XOR8 = 0x128, ROR4 = 0x124, ROR2 = 0x122, ROR1 = 0x121, ROR12 = 0x12C;
#define QUAD(w) u32x4{dppu<0x00>(w), dppu<0x55>(w), dppu<0xAA>(w), dppu<0xFF>(w)}
__device__ __forceinline__ float swap32_add(float a, float b) {
  auto s = __builtin_amdgcn_permlane32_swap(f2u(a), f2u(b), false, false);
  return u2f(s[0]) + u2f(s[1]);
}
__device__ __forceinline__ float swap16_add(float a, float b) {
  auto s = __builtin_amdgcn_permlane16_swap(f2u(a), f2u(b), false, false);
  return u2f(s[0]) + u2f(s[1]);
}
__device__ __forceinline__ float cross_rows_sum(float x) { x = swap16_add(x, x); return swap32_add(x, x); }
__device__ __forceinline__ float cross_rows_max(float x) {
  auto s = __builtin_amdgcn_permlane16_swap(f2u(x), f2u(x), false, false);
  x = fmaxf(u2f(s[0]), u2f(s[1]));
  auto s2 = __builtin_amdgcn_permlane32_swap(f2u(x), f2u(x), false, false);
  return fmaxf(u2f(s2[0]), u2f(s2[1]));
}
__device__ __forceinline__ float wave_sum(float x) {
  x += dpp<XOR8>(x); x += dpp<ROR4>(x); x += dpp<ROR2>(x); x += dpp<ROR1>(x);
  return cross_rows_sum(x);
}
__device__ __forceinline__ float wave_max(float x) {
  x = fmaxf(x, dpp<XOR8>(x)); x = fmaxf(x, dpp<ROR4>(x)); x = fmaxf(x, dpp<ROR2>(x)); x = fmaxf(x, dpp<ROR1>(x));
  return cross_rows_max(x);
}

__device__ __forceinline__ void st_gran(__amdgpu_buffer_rsrc_t rs, u32 off, u32 a, u32 b, u32 tg) {
  u32x4 v = {a, tg, b, tg};
  __builtin_amdgcn_raw_buffer_store_b128(v, rs, off, 0, 16);
}
__device__ __forceinline__ u32x4 ld_gran(__amdgpu_buffer_rsrc_t rs, u32 off) {
  return __builtin_amdgcn_raw_buffer_load_b128(rs, off, 0, 16);
}
constexpr unsigned SPIN_CAP = 1u << 18;
template <int N>
__device__ __forceinline__ void gather(__amdgpu_buffer_rsrc_t rs, const u32 (&offs)[N], u32x4 (&g)[N], u32 tg) {
#pragma nounroll
  for (unsigned spins = 0; spins < SPIN_CAP; ++spins) {
    bool ok = true;
#pragma unroll
    for (int i = 0; i < N; ++i) g[i] = ld_gran(rs, offs[i]);
#pragma unroll
    for (int i = 0; i < N; ++i) ok &= (offs[i] == OOB) | ((g[i][1] == tg) & (g[i][3] == tg));
    if (__all(ok)) break;
    __builtin_amdgcn_s_sleep(1);
  }
}
template <int N, int SLEEP = 1>
__device__ __forceinline__ void gather_m(__amdgpu_buffer_rsrc_t rs, u32 (&offs)[N], u32 (&va)[N], u32 (&vb)[N], u32 tg) {
#pragma unroll
  for (int i = 0; i < N; ++i) { va[i] = 0; vb[i] = 0; }
#pragma nounroll
  for (unsigned spins = 0; spins < SPIN_CAP; ++spins) {
    u32x4 g[N];
#pragma unroll
    for (int i = 0; i < N; ++i) g[i] = ld_gran(rs, offs[i]);
    bool ok = true;
#pragma unroll
    for (int i = 0; i < N; ++i) {
      const bool hit = (offs[i] != OOB) & (g[i][1] == tg) & (g[i][3] == tg);
      va[i] = hit ? g[i][0] : va[i]; vb[i] = hit ? g[i][2] : vb[i];
      offs[i] = hit ? OOB : offs[i];
      ok &= (offs[i] == OOB);
    }
    if (__all(ok)) break;
    __builtin_amdgcn_s_sleep(SLEEP);
  }
}

__device__ __forceinline__ void st_g22(__amdgpu_buffer_rsrc_t rs, u32 off, f32x4 v, u32 tg) {
  const u32 a0 = f2u(v[0]) + 0x200u, a1 = f2u(v[1]) + 0x200u, a2 = f2u(v[2]) + 0x200u, a3 = f2u(v[3]) + 0x200u;
  const u32x4 w = {(a0 >> 10) | ((a1 << 12) & 0xffc00000u), (a1 >> 20) | (tg << 12), (a2 >> 10) | ((a3 << 12) & 0xffc00000u), (a3 >> 20) | (tg << 12)};
  __builtin_amdgcn_raw_buffer_store_b128(w, rs, off, 0, 16);
}
__device__ __forceinline__ f32x4 un_g22(u32x4 w) {
  return f32x4{u2f(w[0] << 10), u2f(__builtin_amdgcn_alignbit(w[1], w[0], 22) << 10), u2f(w[2] << 10), u2f(__builtin_amdgcn_alignbit(w[3], w[2], 22) << 10)};
}
template <int N, int SLEEP>
__device__ __forceinline__ void gather22(__amdgpu_buffer_rsrc_t rs, u32 (&offs)[N], u32x4 (&v)[N], u32 tg) {
#pragma unroll
  for (int i = 0; i < N; ++i) v[i] = u32x4{0u, 0u, 0u, 0u};
#pragma nounroll
  for (unsigned spins = 0; spins < SPIN_CAP; ++spins) {
    u32x4 g[N];
#pragma unroll
    for (int i = 0; i < N; ++i) g[i] = ld_gran(rs, offs[i]);
    bool ok = true;
#pragma unroll
    for (int i = 0; i < N; ++i) {
      const bool hit = (offs[i] != OOB) & (((g[i][1] ^ (tg << 12)) | (g[i][3] ^ (tg << 12))) < 4096u);
#pragma unroll
      for (int k = 0; k < 4; ++k) v[i][k] = hit ? g[i][k] : v[i][k];
      offs[i] = hit ? OOB : offs[i];
      ok &= (offs[i] == OOB);
    }
    if (__all(ok)) break;
    __builtin_amdgcn_s_sleep(SLEEP);
  }
}

typedef __attribute__((address_space(1))) u64* gu64p;
typedef __attribute__((address_space(1))) float* gf32p;
__device__ __forceinline__ void and_u64(unsigned char* p, u64 v) { (void)__hip_atomic_fetch_and((gu64p)p, v, __ATOMIC_RELAXED, __HIP_MEMORY_SCOPE_AGENT); }
__device__ __forceinline__ void add_f32(unsigned char* p, float v) { (void)__builtin_amdgcn_global_atomic_fadd_f32((gf32p)p, v); }
__device__ __forceinline__ u64 fix40(float t) {
  const u32 lo = (u32)(__builtin_amdgcn_fractf(t) * 4294967296.f);
  int fl;
  asm("v_cvt_flr_i32_f32 %0, %1" : "=v"(fl) : "v"(t));
  const u32 hi = (u32)fl + (1u << 22);
  return ((u64)hi << 32) | lo;
}

template <int SL>
__device__ __forceinline__ void pace_wait(__amdgpu_buffer_rsrc_t rs, u32 off, u32 tgt, u64 (&k)[3], u32& clk) {
  u32 n;
#define PW1(x, y) "v_mov_b32 v" #x ", 0\n\tv_mov_b32 v" #y ", 0\n\tbuffer_atomic_add_x2 v[" #x ":" #y "], %5, %7, 0 offen sc0\n\t"
#define PW2(x, y) "s_waitcnt vmcnt(2)\n\tv_cmp_lt_u32 vcc, v" #x ", %6\n\tv_mov_b32 %3, v" #y "\n\ts_cbranch_vccz 2f\n\t" PW1(x, y)
  asm volatile(PW1(190, 191) "s_sleep %8\n\t" PW1(192, 193) "s_sleep %8\n\t" PW1(194, 195) "s_mov_b32 %4, 0x20000\n1:\n\t"
               PW2(190, 191) PW2(192, 193) PW2(194, 195) "s_sub_u32 %4, %4, 1\n\ts_cbranch_scc0 1b\n2:"
               : "=&{v[190:191]}"(k[0]), "=&{v[192:193]}"(k[1]), "=&{v[194:195]}"(k[2]), "=&v"(clk), "=&s"(n) : "v"(off), "v"(tgt), "s"(rs), "n"(SL) : "vcc", "scc", "memory");
}
#define PACE_KEEP(k) asm volatile("" :: "{v[190:191]}"(k[0]), "{v[192:193]}"(k[1]), "{v[194:195]}"(k[2]))
__device__ __forceinline__ void pace_due(u32 clk, u32 d) {
  clk = max(clk, dppu<XOR1>(clk)); clk = max(clk, dppu<XOR2>(clk));
  const u32 due = __builtin_amdgcn_readfirstlane(clk) + d;
#pragma nounroll
  for (int spins = 0; spins < 64; ++spins) if ((int)((u32)__builtin_amdgcn_s_memrealtime() - due) >= 0) break;
}
__device__ __forceinline__ void pace_post(__amdgpu_buffer_rsrc_t rs, u32 off, u32 now) {
  asm volatile("buffer_atomic_umax %0, %2, %3, 0 offen offset:4\n\tbuffer_atomic_add %1, %2, %3, 0 offen" :: "v"(now), "v"(1u), "v"(off), "s"(rs) : "memory");
}

__device__ __forceinline__ u32 next_tag(u32 ep) { const u32 t = ep + 1u; return (t & 0xfffffu) ? t : t + 1u; }

__device__ __forceinline__ void lds_arrive(u32 addr, int lane) {
  asm volatile("s_waitcnt lgkmcnt(0)" ::: "memory");
  if (lane == 0) asm volatile("ds_add_u32 %0, %1" :: "v"(addr), "v"(1u) : "memory");
}
__device__ __forceinline__ u32 lds_ticket(u32 addr, int lane) {
  u32 r = 0;
  asm volatile("s_waitcnt lgkmcnt(0)" ::: "memory");
  if (lane == 0) asm volatile("ds_add_rtn_u32 %0, %1, %2\n\ts_waitcnt lgkmcnt(0)" : "=v"(r) : "v"(addr), "v"(1u) : "memory");
  return __builtin_amdgcn_readfirstlane(r);
}
template <int SLEEP, int TARGET>
__device__ __forceinline__ void lds_wait_(u32 addr) {
  u32 v, n, w;
  asm volatile("s_mov_b32 %1, 0x400000\n1:\n\tds_read_b32 %0, %3\n\ts_waitcnt lgkmcnt(0)\n\tv_readfirstlane_b32 %2, %0\n\ts_cmp_ge_u32 %2, %4\n\ts_cbranch_scc1 2f\n\t"
               "s_sleep %5\n\ts_sub_u32 %1, %1, 1\n\ts_cbranch_scc0 1b\n2:" : "=&v"(v), "=&s"(n), "=&s"(w) : "v"(addr), "n"(TARGET), "n"(SLEEP) : "scc", "memory");
}
#define LDS_WAIT(SLEEP, addr, target) lds_wait_<SLEEP, target>(addr)
#define TOUCH(l) do { u64 ex_; asm volatile("s_mov_b64 %0, exec\n\ts_mov_b64 exec, 0\n\ts_branch " l "\n" l "r:\n\ts_mov_b64 exec, %0" : "=&s"(ex_)); } while (0)
#define PAD(l) asm volatile(l ":\n\ts_cbranch_execz " l "r")
#define PAD0(l) asm volatile(l ":\n\ts_cbranch_execz .Ltr")
__device__ __forceinline__ void dma16(const void* gsrc, u32 lds_dst) {
  u32 keep;
  asm volatile("s_mov_b32 %0, m0\n\ts_mov_b32 m0, %2\n\ts_nop 0\n\tglobal_load_lds_dwordx4 %1, off\n\ts_mov_b32 m0, %0"
               : "=&s"(keep) : "v"(gsrc), "s"(lds_dst) : "memory");
}
template <int N, int GS>
__device__ __forceinline__ void dma_run(const void* base, u32 off, u32 lds_dst) {
  u32 keep;
  asm volatile("s_mov_b32 %0, m0\n\ts_mov_b32 m0, %3\n\ts_nop 0\n\tglobal_load_lds_dwordx4 %1, %2 nt\n\t"
               ".rept %6\n\ts_add_u32 m0, m0, %5\n\tv_add_u32 %1, %4, %1\n\tglobal_load_lds_dwordx4 %1, %2 nt\n\t.endr\n\ts_mov_b32 m0, %0"
               : "=&s"(keep), "+v"(off) : "s"(base), "s"(lds_dst), "s"(GS), "s"(4096), "n"(N - 1) : "memory", "scc");
}
__device__ __forceinline__ void dma4(const void* base, u32 off, u32 lds_dst) {
  u32 keep;
  asm volatile("s_mov_b32 %0, m0\n\ts_mov_b32 m0, %3\n\ts_nop 0\n\tglobal_load_lds_dwordx4 %1, %2\n\tglobal_load_lds_dwordx4 %1, %2 offset:1024\n\t"
               "global_load_lds_dwordx4 %1, %2 offset:2048\n\tglobal_load_lds_dwordx4 %1, %2 offset:3072\n\ts_mov_b32 m0, %0"
               : "=&s"(keep) : "v"(off), "s"(base), "s"(lds_dst) : "memory");
}
__device__ __forceinline__ void dma2(const void* base, u32 off, u32 lds_dst) {
  u32 keep;
  asm volatile("s_mov_b32 %0, m0\n\ts_mov_b32 m0, %3\n\ts_nop 0\n\tglobal_load_lds_dwordx4 %1, %2\n\tglobal_load_lds_dwordx4 %1, %2 offset:1024\n\ts_mov_b32 m0, %0"
               : "=&s"(keep) : "v"(off), "s"(base), "s"(lds_dst) : "memory");
}
__device__ __forceinline__ void dma5(const void* base, u32 off, u32 off2, u32 lds_dst) {
  u32 keep;
  asm volatile("s_mov_b32 %0, m0\n\ts_mov_b32 m0, %3\n\ts_nop 0\n\tglobal_load_lds_dwordx4 %1, %2\n\tglobal_load_lds_dwordx4 %1, %2 offset:1024\n\t"
               "global_load_lds_dwordx4 %1, %2 offset:2048\n\tglobal_load_lds_dwordx4 %1, %2 offset:3072\n\tglobal_load_lds_dwordx4 %4, %2\n\ts_mov_b32 m0, %0"
               : "=&s"(keep) : "v"(off), "s"(base), "s"(lds_dst), "v"(off2) : "memory");
}
template <class T>
__device__ __forceinline__ void ld_row(const void* ws, const void* base, u32 zero, u32 o0, u32 o1, u32& ep, u32x4& v0, T& v1) {
  if constexpr (sizeof(T) == 4)
    asm volatile("global_load_dword %0, %3, %6 sc1\n\tglobal_load_dwordx4 %1, %4, %7\n\tglobal_load_dword %2, %5, %7 offset:1024"
                 : "=&v"(ep), "=&v"(v0), "=&v"(v1) : "v"(zero), "v"(o0), "v"(o1), "s"(ws), "s"(base) : "memory");
  else
    asm volatile("global_load_dword %0, %3, %6 sc1\n\tglobal_load_dwordx4 %1, %4, %7\n\tglobal_load_dwordx4 %2, %5, %7 offset:1024"
                 : "=&v"(ep), "=&v"(v0), "=&v"(v1) : "v"(zero), "v"(o0), "v"(o1), "s"(ws), "s"(base) : "memory");
}
template <int D2>
__device__ __forceinline__ s16x8 wfrag(u32 addr) {
  const s16x4 lo = __builtin_amdgcn_ds_read_tr16_b64_v4i16((lds_s4p)(uintptr_t)addr);
  const s16x4 hi = __builtin_amdgcn_ds_read_tr16_b64_v4i16((lds_s4p)(uintptr_t)(addr + D2));
  return s16x8{lo[0], lo[1], lo[2], lo[3], hi[0], hi[1], hi[2], hi[3]};
}
template <int D2, int D4>
__device__ __forceinline__ s16x16 wfrag2(u32 addr) {
  const s16x8 lo = wfrag<D2>(addr), hi = wfrag<D2>(addr + D4);
  return __builtin_shufflevector(lo, hi, 0, 1, 2, 3, 4, 5, 6, 7, 8, 9, 10, 11, 12, 13, 14, 15);
}
#define SMFMA(x_, w_, c_, i_) __builtin_amdgcn_smfmac_f32_16x16x64_bf16(__builtin_bit_cast(bf16x8v, x_), __builtin_bit_cast(bf16x16v, w_), c_, i_, 0, 0)
#define MFMA(w_, x_, c_) __builtin_amdgcn_mfma_f32_16x16x32_bf16(__builtin_bit_cast(bf16x8v, w_), __builtin_bit_cast(bf16x8v, x_), c_, 0, 0, 0)

struct Args {
  const bf16 *x, *kc, *vc, *wq, *wo, *w1, *b1, *w2, *b2, *c, *wmod;
  bf16* out;
  unsigned char* ws;
};

constexpr int L_W = 0;
constexpr int L_XA = 131072;
constexpr int XS = 1312;
constexpr int L_RED = L_XA + 12 * XS;
constexpr int L_XR = L_RED;
constexpr int L_PT = L_RED + 4096;
constexpr int L_OB = L_PT + 2048;
constexpr int L_QS = L_OB + 8 * 272 + 256;
constexpr int L_ST = L_QS + 512;
constexpr int L_CNT = L_ST + 256;
constexpr int L_END = L_CNT + 64;
static_assert(L_END <= 160 * 1024, "LDS");
constexpr u32 C_Q = L_CNT, C_SC = L_CNT + 4, C_ISS = L_CNT + 8, C_P1 = L_CNT + 20, C_P2 = L_CNT + 24, C_GO = L_CNT + 28, C_ML = L_CNT + 12, C_FIN = L_CNT + 32, C_TK = L_CNT + 36, C_TF = L_CNT + 40, C_WO = L_CNT + 48, C_KI = L_CNT + 44;

constexpr int NL = 32;
#define MXS 694,258,692,174,758,930,660,128,114,1337,1980,536,600,1760,903,1065,261,2005
#define TD_ 3
#define D1_ 50
#define D2_ 76
#define CW_ 1
constexpr int NP = 8;
constexpr int NV0 = 10;
constexpr int NV1 = 14;
constexpr int PVLAG = 14;
static_assert(NV1 == PVLAG && NV1 + 2 == NL - 2 - PVLAG, "row groups");

constexpr u32 MXC[18] = {MXS};
constexpr u32 slot_adr(int i) { u32 v = 0; for (int k = 0; k < 5; ++k) v ^= ((i >> k) & 1) ? MXC[6 + k] : 0u; return v << 8; }
constexpr bool mx_ok() {
  u32 b[11] = {}; int n = 0;
  for (int k = 0; k < 11; ++k) { u32 v = MXC[k]; for (int j = 0; j < n; ++j) v = (v ^ b[j]) < v ? v ^ b[j] : v; if (!v || v > 2047) return false; b[n++] = v; }
  for (int k = 11; k < 18; ++k) if (MXC[k] > 2047) return false;
  return true;
}
static_assert(mx_ok(), "every key row exactly once");
__device__ __forceinline__ u32 lane_adr(int u, int wave, int lane) {
  u32 ms = 0;
#pragma unroll
  for (int k = 0; k < 3; ++k) ms ^= ((wave >> k) & 1) ? MXC[2 + k] << 8 : 0u;
  ms ^= (u >> 7) ? MXC[5] << 8 : 0u;
#pragma unroll
  for (int k = 0; k < 7; ++k) ms ^= ((u >> k) & 1) ? MXC[11 + k] << 8 : 0u;
  return (((lane & 15) * 16) | (((lane & 16) ? MXC[0] << 8 : 0u) ^ ((lane & 32) ? MXC[1] << 8 : 0u))) ^ ms;
}

__global__ void __launch_bounds__(NT, 2) mega_kernel(Args a) {
  __shared__ __attribute__((aligned(1024))) unsigned char lds[L_END];
  u32* xraw = reinterpret_cast<u32*>(lds + L_XR);
  u32* pT = reinterpret_cast<u32*>(lds + L_PT);
  u32* qs = reinterpret_cast<u32*>(lds + L_QS);
  float* st = reinterpret_cast<float*>(lds + L_ST);
  u32* cnt = reinterpret_cast<u32*>(lds + L_CNT);

  const int t = threadIdx.x, lane = t & 63;
  const int wave = __builtin_amdgcn_readfirstlane(t >> 6);
  const int u = blockIdx.x;
  {
    u64 ex, pc; u32 t0, t1, va;
    asm volatile(
      "s_cmp_lt_u32 %[w], 4\n\ts_cbranch_scc1 2f\n\t"
      "s_mov_b64 %[ex], exec\n\t"
      "s_sub_u32 %[t0], %[w], 5\n\ts_cmp_lt_u32 %[t0], 2\n\ts_cbranch_scc1 1f\n\t"
      "s_mov_b64 exec, 0\n\t"
      "s_cmp_eq_u32 %[w], 7\n\ts_cbranch_scc1 .Lt2\n\t"
      "s_bitcmp1_b32 %[u], 0\n\ts_cbranch_scc1 .Lt1\n\t"
      "s_branch .Lt0\n"
      "1:\n\t"
      "s_getpc_b64 %[pc]\n"
      ".Lpf0:\n\t"
      "s_lshl_b32 %[t0], %[t0], 15\n\ts_lshl_b32 %[t1], %[u], 7\n\ts_and_b32 %[t1], %[t1], 0x7c00\n\ts_or_b32 %[t0], %[t0], %[t1]\n\t"
      "v_lshl_or_b32 %[va], %[ln], 4, %[t0]\n\tv_min_u32 %[va], (.Lpf1-.Lpf0-16)&~15, %[va]\n\t"
      "s_mov_b32 %[t1], m0\n\ts_lshl_b32 m0, %[w], 14\n\ts_andn2_b64 %[pc], %[pc], 127\n\t"
      "s_cmp_lt_u32 %[t0], .Lpf1-.Lpf0\n\ts_cselect_b64 exec, exec, 0\n\t"
      "global_load_lds_dwordx4 %[va], %[pc]\n\t"
      "s_mov_b32 m0, %[t1]\n\ts_mov_b64 exec, 0\n\t"
      "s_cmp_eq_u32 %[w], 5\n\ts_cbranch_scc1 .Ltr\n\t"
      "s_bitcmp0_b32 %[u], 0\n\ts_cbranch_scc1 .Lt5\n\t"
      "s_bitcmp0_b32 %[u], 7\n\ts_cbranch_scc1 .Lt6\n\t"
      "s_branch .Lt3\n"
      ".Ltr:\n\t"
      "s_mov_b64 exec, %[ex]\n"
      "2:"
      : [ex] "=&s"(ex), [pc] "=&s"(pc), [t0] "=&s"(t0), [t1] "=&s"(t1), [va] "=&v"(va) : [w] "s"(wave), [u] "s"(u), [ln] "v"(lane) : "scc");
  }
  const int half = u & 1, b_att = (u >> 5) & 3, h_att = u & 31, bh = b_att * 32 + h_att, ahalf = u >> 7;
  const int r = lane >> 4, c = lane & 15;
#define WS_RSRC unsigned char* wsp_ = a.ws; asm volatile("" : "+s"(wsp_)); const __amdgpu_buffer_rsrc_t rs = __builtin_amdgcn_make_buffer_rsrc(wsp_, 0, WS_END, 0x00020000)
  STAMP_DECL
  STAMPT(0, 0);
  if (t < 16) cnt[t] = 0;
  u32 tg;
  const u32 tr128 = (8 * r + (c >> 2)) * 128 + ((((c >> 3) & 1) | ((r & 1) << 1)) * 32) + (c & 3) * 8;
  const int s128e = (((((lane & 7) >> 1) ^ ((lane >> 4) & 1)) << 1) | (lane & 1)) * 8;
  const int g = u >> 1, ksp = g & 3, cbp = g >> 2;
  const int xc = g & 3, pi = b_att * 4 + ((u >> 3) & 3);
  const int cb = xc < 2 ? 2 * pi + xc : 30 + 2 * pi + xc;
  const int bm = xc == 3 ? 3 : 7;
  const bool bpair = half && (pi & bm) != bm;
  const int ji = pi - (pi >> (xc == 3 ? 2 : 3));
  const int jB = 14 * xc + ji;
  u32x4 kv[NL];
#pragma unroll
  for (int i = NL - NP; i < NL; ++i) asm volatile("" : "=v"(kv[i]));
  if (wave < 4) {
    __builtin_amdgcn_s_setprio(3);
    if (half) {
      if (cb < 60 && !ahalf) {
        u32 om = ((8 * wave + (lane >> 3)) * MODN + cb * 64 + (s128e ^ ((wave & 1) * 32))) * 2, oc0 = wave * 1280 + lane * 16, oc1 = wave * 1280 + lane * 4, oz = WS_EPOCH;
        u32 ld = L_W + wave * 1024;
        const u32 a0 = cb < 5 ? (((((4 * cb + wave) / 5) & 1) ? 12u : 15u) << 22) + (1u << 21) + (1u << 31) : 0u;
        u32x4 clr = {0u, a0, 0u, a0};
        static_assert(G_OUT - G_ACC <= 20 * 4096 && (G_OUT - G_ACC) % 4096 == 0, "cleared by blocks the q projection waits for");
        u32 ocl = cb < (G_OUT - G_ACC) / 4096 ? G_ACC + (u32)(4 * cb + wave) * 1024 + lane * 16 : OOB;
        asm volatile("" : "+v"(om), "+v"(oc0), "+v"(oc1), "+v"(oz), "+s"(ld), "+v"(clr), "+v"(ocl));
        u32 ep, cw;
        u32x4 cv;
        ld_row(a.ws, a.c, oz, oc0, oc1, ep, cv, cw);
        dma_run<20, 32 * MODN * 2>(a.wmod, om, ld);
        WS_RSRC;
        __builtin_amdgcn_raw_buffer_store_b128(clr, rs, ocl, 0, 16);
        BAR();
        PAD0(".Lt1");
        asm volatile("s_waitcnt vmcnt(21)" : "+v"(ep), "+v"(cv), "+v"(cw) :: "memory");
        u32x4 sv;
#pragma unroll
        for (int k = 0; k < 4; ++k) {
          const float c0 = lo16(cv[k]), c1 = hi16(cv[k]);
          sv[k] = cvt2(c0 * frcp(1.f + fexp(-c0)), c1 * frcp(1.f + fexp(-c1)));
        }
        *reinterpret_cast<u32x4*>(lds + L_XA + wave * XS + lane * 16) = sv;
        *reinterpret_cast<u32*>(lds + L_XA + wave * XS + 1024 + lane * 4) = cvt2(lo16(cw) * frcp(1.f + fexp(-lo16(cw))), hi16(cw) * frcp(1.f + fexp(-hi16(cw))));
        if (t == 0) cnt[4] = next_tag(ep);
        STAMPT(2, 0);
        lds_arrive(C_P2, lane);
        LDS_WAIT(1, C_P2, 4);
        u32x4 av[20];
#pragma unroll
        for (int s = 0; s < 20; ++s) av[s] = *reinterpret_cast<const u32x4*>(lds + L_XA + (c & 3) * XS + s * 64 + r * 16);
        tg = __builtin_amdgcn_readfirstlane(cnt[4]);
#pragma unroll
        for (int s = 0; s < 20; ++s) asm volatile("" : "+v"(av[s]));
        PAD0(".Lt6");
        VM0();
        STAMPT(5, 128);
        lds_arrive(C_P1, lane);
        LDS_WAIT(1, C_P1, 4);
        f32x4 acc[2] = {{0.f, 0.f, 0.f, 0.f}, {0.f, 0.f, 0.f, 0.f}};
#pragma unroll
        for (int s = 0; s < 20; ++s) acc[s & 1] = MFMA(wfrag<512>(L_W + (tr128 ^ (wave * 32)) + s * 4096), av[s], acc[s & 1]);
        acc[0] += acc[1];
        st_gran(rs, c >= 4 ? OOB : cb < 20 ? G_MOD + cb * 1024 + wave * 256 + r * 64 + c * 16 : G_MOD + cb * 1024 + wave * 256 + (r >> 1) * 128 + c * 32 + (r & 1) * 16, cvt2(acc[0][0], acc[0][1]), cvt2(acc[0][2], acc[0][3]), tg);
      } else {
        if (ahalf) {
          const u32 l0 = ((u32)(uintptr_t)a.wmod >> 7) + cb;
          if ((l0 & 3) == 3 && cb < 60) {
            const u32 oh = ((256 + 2 * (8 * wave + (lane >> 3)) + ((l0 >> 2) & 1)) * MODN + cb * 64 + (lane & 7) * 8) * 2;
#pragma unroll
            for (int i = 0; i < 6; ++i) dma16((const unsigned char*)a.wmod + oh + i * (64 * MODN * 2), L_W + wave * 16384);
          }
          const bf16* po = a.wo + (h_att * 128 + b_att * 32) * DM + wave * 5120;
          dma4(po, lane * 16, L_W + wave * 16384);
          dma4(po, lane * 16 + 4096, L_W + wave * 16384);
          dma2(po, lane * 16 + 8192, L_W + wave * 16384);
        }
        BAR();
        if (wave == 0) {
          WS_RSRC;
          tg = __builtin_amdgcn_readfirstlane(next_tag(__builtin_amdgcn_raw_buffer_load_b32(rs, WS_EPOCH, 0, 16)));
          if (t == 0) cnt[4] = tg;
        }
      }
      lds_arrive(C_GO, lane);
      lds_arrive(C_TF, lane);
      STAMPT(9, 0);
    } else {
      u32 oq = ((ksp * 160 + 4 * wave + r) * QD + cbp * 128 + (c & 1) * 8 + ((((c >> 1) ^ r) * 16) ^ ((wave >> 1) * 64))) * 2;
      u32 ox0 = wave * 1280 + lane * 16, ox1 = wave * 1280 + c * 16, oz = WS_EPOCH, ld = L_W + wave * 1024;
      asm volatile("" : "+v"(oq), "+v"(ox0), "+v"(ox1), "+v"(oz), "+s"(ld));
      u32 ep;
      u32x4 xv[2];
      ld_row(a.ws, a.x, oz, ox0, ox1, ep, xv[0], xv[1]);
      dma_run<10, 16 * QD * 2>(a.wq, oq, ld);
      BAR();
      asm volatile("s_waitcnt vmcnt(10)" : "+v"(ep), "+v"(xv[0]), "+v"(xv[1]) :: "memory");
      {
        float xf[16];
#pragma unroll
        for (int k = 0; k < 4; ++k) { xf[2 * k] = lo16(xv[0][k]); xf[2 * k + 1] = hi16(xv[0][k]); xf[8 + 2 * k] = lo16(xv[1][k]); xf[8 + 2 * k + 1] = hi16(xv[1][k]); }
        float s = 0.f, s2 = 0.f;
#pragma unroll
        for (int k = 0; k < 8; ++k) { s += xf[k]; s2 += xf[8 + k]; }
        if (lane < 16) s += s2;
        const float mean = wave_sum(s) * (1.f / DM);
        float v = 0.f, v2 = 0.f;
#pragma unroll
        for (int k = 0; k < 8; ++k) { const float d = xf[k] - mean; v += d * d; const float d2 = xf[8 + k] - mean; v2 += d2 * d2; }
        if (lane < 16) v += v2;
        v = wave_sum(v);
        if (lane == 0) { st[24 + 2 * wave] = bfr(mean); st[24 + 2 * wave + 1] = frcp(bfr(fsqrt(bfr(bfr(v * (1.f / DM)) + 1e-5f)))); }
        *reinterpret_cast<u32x4*>(xraw + wave * 320 + lane * 4) = xv[0];
        if (lane < 16) *reinterpret_cast<u32x4*>(xraw + wave * 320 + 256 + lane * 4) = xv[1];
        if (t == 0) cnt[4] = next_tag(ep);
      }
      PAD0(".Lt0");
      VM0();
      lds_arrive(C_P1, lane);
      if (wave < 2) {
        LDS_WAIT(1, C_P1, 4);
        s16x8 wq[4][5];
#pragma unroll
        for (int e = 0; e < 4; ++e) {
          const u32 tr256 = (8 * r + (c >> 2)) * 256 + ((((c >> 2) | ((r & 1) << 2)) ^ (4 * wave + e)) * 32) + (c & 3) * 8;
#pragma unroll
          for (int s = 0; s < 5; ++s) wq[e][s] = wfrag<1024>(L_W + tr256 + s * 8192);
        }
#pragma unroll
        for (int e = 0; e < 4; ++e) asm volatile("" : "+v"(wq[e][0]), "+v"(wq[e][1]), "+v"(wq[e][2]), "+v"(wq[e][3]), "+v"(wq[e][4]));
        lds_arrive(C_TF, lane);
        tg = __builtin_amdgcn_readfirstlane(cnt[4]);
        WS_RSRC;
        u32 offs[4], ma[4], mb[4];
        float nn[2][4];
#pragma unroll
        for (int i = 0; i < 2; ++i) {
          const int it = t + 128 * i, bb = it & 3, j = it < 160 ? it >> 2 : 0;
#pragma unroll
          for (int sel = 0; sel < 2; ++sel) offs[2 * i + sel] = it >= 160 ? OOB : G_MOD + (sel * 640 + ksp * 160 + it) * 16;
          const float mean = st[24 + 2 * bb], sd = st[24 + 2 * bb + 1];
          const u32x2 xw = *reinterpret_cast<const u32x2*>(xraw + bb * 320 + ksp * 80 + 2 * j);
#pragma unroll
          for (int k = 0; k < 4; ++k) {
            const float xf = (k & 1) ? hi16(xw[k >> 1]) : lo16(xw[k >> 1]);
            nn[i][k] = bfr(bfr(xf - mean) * sd);
            asm volatile("" : "+v"(nn[i][k]));
          }
        }
        u32 o_q = G_Q + ((ksp * 4 + (c >> 2)) * 1024 + cbp * 32 + wave * 16 + r * 4 + (c & 3)) * 16;
        asm volatile("" : "+v"(o_q));
        {
          int u2 = u, w2 = wave, l2 = lane;
          asm volatile("" : "+s"(u2), "+s"(w2), "+v"(l2));
          const u32 m0 = lane_adr(u2, w2, l2);
          const __amdgpu_buffer_rsrc_t rk0 = __builtin_amdgcn_make_buffer_rsrc((void*)a.kc, 0, 1u << 28, 0x00020000);
#pragma unroll
          for (int i = NL - NP; i < NL; ++i) { kv[i] = __builtin_amdgcn_raw_buffer_load_b128(rk0, m0 ^ slot_adr(i), (u32)((u2 & 127) * SK + (SK - WIN)) * 256u, 2); asm volatile("" ::: "memory"); }
        }
        gather_m<4, 4>(rs, offs, ma, mb, tg);
        PAD0(".Lt5");
        STAMPT(8, 0);
#pragma unroll
        for (int i = 0; i < 2; ++i) {
          const int it = t + 128 * i;
          const f32x4 sa = {lo16(ma[2 * i]), hi16(ma[2 * i]), lo16(mb[2 * i]), hi16(mb[2 * i])};
          const f32x4 ca = {lo16(ma[2 * i + 1]), hi16(ma[2 * i + 1]), lo16(mb[2 * i + 1]), hi16(mb[2 * i + 1])};
          float hv[4];
#pragma unroll
          for (int k = 0; k < 4; ++k) hv[k] = bfr(nn[i][k] * bfr(1.f + ca[k])) + sa[k];
          *reinterpret_cast<u32x2*>(lds + L_XA + (it < 160 ? (it & 3) * 320 + (it >> 2) * 8 : it * 8)) = u32x2{cvt2(hv[0], hv[1]), cvt2(hv[2], hv[3])};
        }
        lds_arrive(C_P2, lane);
        LDS_WAIT(1, C_P2, 2);
        u32x4 av[5];
#pragma unroll
        for (int s = 0; s < 5; ++s) av[s] = *reinterpret_cast<const u32x4*>(lds + L_XA + (c >> 2) * 320 + s * 64 + r * 16);
        f32x4 acc[4] = {{0.f, 0.f, 0.f, 0.f}, {0.f, 0.f, 0.f, 0.f}, {0.f, 0.f, 0.f, 0.f}, {0.f, 0.f, 0.f, 0.f}};
#pragma unroll
        for (int s = 0; s < 5; ++s)
#pragma unroll
          for (int e = 0; e < 4; ++e) acc[e] = MFMA(wq[e][s], av[s], acc[e]);
        const f32x4 a0 = (c & 1) ? acc[1] : acc[0], a1 = (c & 1) ? acc[3] : acc[2];
        st_g22(rs, o_q, (c & 2) ? a1 : a0, tg);
      } else {
        lds_arrive(C_TF, lane);
      }
      PAD0(".Lt2");
      lds_arrive(C_GO, lane);
      STAMPT(9, 0);
    }
    __builtin_amdgcn_s_setprio(0);
  }
  u32 mv = lane_adr(u, wave, lane);
#define KADR(i) (mv ^ slot_adr(i))
#define VADR(i) KADR(i)
  u32 mb = (u32)(bh * SK + (SK - WIN)) * 256u;
  u32 ka[4] = {KADR(0), KADR(1), KADR(2), KADR(3)}, po0 = lane * 16, po1 = lane * 16 + 4096;
  asm volatile("" : "+v"(mv), "+s"(mb), "+v"(ka[1]), "+v"(ka[2]), "+v"(ka[3]), "+v"(po0), "+v"(po1));
  const __amdgpu_buffer_rsrc_t rk = __builtin_amdgcn_make_buffer_rsrc((void*)a.kc, 0, 1u << 28, 0x00020000);
#define LDK(i) __builtin_amdgcn_raw_buffer_load_b128(rk, (i) < 4 ? ka[(i) & 3] : KADR(i), mb, 2)
#define LDV(i) __builtin_amdgcn_raw_buffer_load_b128(rv, VADR(i), mb, 2)
  const bf16* po = a.wo + (h_att * 128 + (u >> 5) * 16) * DM + (wave - 4) * 2560;
  asm volatile("" : "+s"(po) : "s"(rk));
  auto body = [&](auto RB_) __attribute__((always_inline)) {
    constexpr bool RB = decltype(RB_)::value;
    if constexpr (RB) PAD0(".Lt3");
    u32x4 vv[NV0];
    if (wave >= 4) {
      __builtin_amdgcn_s_setprio(2);
      BAR();
      STAMPT(26, 448);
      if (!RB && !half) dma5(po, po0, po1, L_W + wave * 16384);
    }
    const bf16* wp1 = a.w1 + (size_t)(wave * 80 + (lane >> 2)) * FF + jB * 32 + ((((((lane >> 1) & 1) ^ (lane >> 5)) << 1) | (lane & 1)) * 8);
    const bf16* wp2 = a.w2 + (size_t)(jB * 32 + (wave >> 1) * 8 + (lane >> 3)) * DM + (wave & 1) * 320 + (s128e ^ ((wave & 2) * 16));
#pragma unroll
    for (int i = 0; i < NL - NP; ++i) { kv[i] = LDK(i); asm volatile("" ::: "memory"); }
    if (RB || half || wave >= 2) {
#pragma unroll
      for (int i = NL - NP; i < NL; ++i) { kv[i] = LDK(i); asm volatile("" ::: "memory"); }
    }
    if (lane == 0) asm volatile("ds_add_u32 %0, %1" :: "v"(C_KI), "v"(1u) : "memory");
    WS_RSRC;
    const __amdgpu_buffer_rsrc_t rv = __builtin_amdgcn_make_buffer_rsrc((void*)a.vc, 0, 1u << 28, 0x00020000);
    const bf16* wsl = a.wo + (size_t)(h_att * 128 + (lane >> 3)) * DM;
    if constexpr (!RB) {
      if (wave < 5) LDS_WAIT(2, C_TF, 4);
      LDS_WAIT(2, C_KI, 6);
#pragma unroll
      for (int i = 0; i < 16; ++i) dma16(wsl + (size_t)(8 * i) * DM + wave * 64 + (s128e ^ ((i & 1) * 32)), L_W + wave * 16384 + i * 1024);
    }
    STAMPT(25, 448); WSTAMP(0);
    if (wave == 7) {
      LDS_WAIT(2, C_GO, 4);
      tg = __builtin_amdgcn_readfirstlane(cnt[4]);
      u32 offs[2];
      u32x4 gq[2];
#pragma unroll
      for (int e = 0; e < 2; ++e) offs[e] = G_Q + (((2 * (lane >> 5) + e) * 4 + b_att) * 1024 + h_att * 32 + (lane & 31)) * 16;
      __builtin_amdgcn_s_setprio(3);
      gather22<2, 4>(rs, offs, gq, tg);
      STAMPT(10, 448);
      f32x4 q4 = un_g22(gq[0]) + un_g22(gq[1]);
#pragma unroll
      for (int k = 0; k < 4; ++k) q4[k] = swap32_add(q4[k], q4[k]);
      if (lane < 32) *reinterpret_cast<u32x2*>(qs + ((lane & 16) * 2 + (lane & 3) * 8 + ((lane >> 2) & 3) * 2)) = u32x2{cvt2(q4[0], q4[1]), cvt2(q4[2], q4[3])};
      lds_arrive(C_Q, lane);
      __builtin_amdgcn_s_setprio(2);
    }
    LDS_WAIT(2, C_KI, 8);
    STAMPT(29, 320);
#pragma unroll
    for (int i = 0; i < NV0; ++i) { vv[i] = LDV(i); asm volatile("" ::: "memory"); }
    if (wave == 5) {
      if constexpr (RB) { TOUCH(".Lb4"); TOUCH(".Lb3"); TOUCH(".Lb2"); TOUCH(".Lb1"); }
      else { TOUCH(".Lc5"); TOUCH(".Lc4"); TOUCH(".Lc3"); TOUCH(".Lc2"); TOUCH(".Lc0"); TOUCH(".Lc1"); }
    }
    LDS_WAIT(2, C_Q, 1);
    u32x4 qpk;
    asm volatile("ds_read_b128 %0, %2\n\tds_read_b32 %1, %3\n\ts_waitcnt lgkmcnt(0)" : "=&v"(qpk), "=&v"(tg) : "v"((u32)(L_QS + 16 * c)), "v"((u32)(L_CNT + 16)) : "memory");
    tg = __builtin_amdgcn_readfirstlane(tg);
    float x[16];
#pragma unroll
    for (int i = 0; i < 16; ++i) {
      float d0 = 0.f, d1 = 0.f;
#pragma unroll
      for (int k = 0; k < 4; ++k) { d0 = dot2(kv[i][k], qpk[k], d0); d1 = dot2(kv[i + 16][k], qpk[k], d1); }
      const float t1 = d0 + dpp<XOR8>(d0), t2 = d1 + dpp<XOR8>(d1);
      x[i] = (c & 8) ? t2 : t1;
    }
#pragma unroll
    for (int i = 0; i < 16; ++i) asm volatile("" : "+v"(x[i]));
    asm volatile("" ::: "memory");
#pragma unroll
    for (int i = NV0; i < NV1; ++i) { kv[i - NV0] = LDV(i); asm volatile("" ::: "memory"); }
#define TR_STEP(x, HALF, CTRL, BIT) _Pragma("unroll") for (int hh = 0; hh < (HALF); ++hh) { \
    const float lo_ = x[hh], hi_ = x[hh + (HALF)]; const float t1_ = lo_ + dpp<CTRL>(lo_), t2_ = hi_ + dpp<CTRL>(hi_); \
    x[hh] = (c & (BIT)) ? t2_ : t1_; }
    TR_STEP(x, 8, XOR7, 4) TR_STEP(x, 4, XOR2, 2) TR_STEP(x, 2, XOR1, 1)
#undef TR_STEP
    const float s0 = bfr(bfr(x[0]) * 0.08838834764831845f), s1 = bfr(bfr(x[1]) * 0.08838834764831845f);
    const float m = wave_max(fmaxf(s0, s1));
    const float p0 = __expf(s0 - m), p1 = __expf(s1 - m);
    const float l = wave_sum(p0 + p1);
    if (lane == 0) { st[wave] = m; st[8 + wave] = l; }
    pT[(wave * 4 + r) * 16 + c] = cvt2(p0, p1);
    __builtin_amdgcn_s_setprio(0);
    STAMPT(27, 448); WSTAMP(1);
    if (lds_ticket(C_SC, lane) == 7) {
      const float mw = st[lane & 7], lw = st[8 + (lane & 7)];
      float M = fmaxf(mw, dpp<XOR1>(mw)); M = fmaxf(M, dpp<XOR2>(M)); M = fmaxf(M, dpp<XOR7>(M));
      const float e = __expf(mw - M);
      float L = lw * e;
      L += dpp<XOR1>(L); L += dpp<XOR2>(L); L += dpp<XOR7>(L);
      if (lane == 0) { st_gran(rs, G_ML + u * 64, f2u(M), f2u(L), tg); st[40] = M; st[41] = L; }
      if (lane < 8) st[16 + lane] = e;
      lds_arrive(C_ML, lane);
      if (!RB) asm volatile("s_waitcnt vmcnt(%0)" :: "n"(NV1 + 1) : "memory");
    } else {
      if (!RB) asm volatile("s_waitcnt vmcnt(%0)" :: "n"(NV1) : "memory");
    }
    s16x16 wf[2][4];
    const int kp = 4 * r + (c & 3);
    const int k0 = (c >> 2) * DM + wave * 80 + 8 * r + 2 * (c & 3);
    u32 o_m, o_m2, b1w, xw[3];
    u32x2 gm[9];
#define LD_GM _Pragma("unroll") for (int i = 0; i < 9; ++i) gm[i] = __builtin_amdgcn_raw_buffer_load_b64(rs, (i % 3 == 2 ? o_m2 : o_m + (i % 3) * 512) + (i / 3) * 10240, 0, 16)
    if constexpr (RB) {
      o_m = G_MF + wave * 1280 + lane * 8; o_m2 = lane < 32 ? o_m + 1024 : OOB;
      LD_GM;
      b1w = *reinterpret_cast<const u32*>(a.b1 + jB * 32 + 2 * kp);
#pragma unroll
      for (int i = 0; i < 3; ++i) xw[i] = *reinterpret_cast<const u32*>(a.x + k0 + (i < 2 ? 32 * i : lane < 32 ? 64 : 0));
      asm volatile("" ::: "memory");
    } else {
#pragma unroll
      for (int s = 0; s < 2; ++s)
#pragma unroll
        for (int nt = 0; nt < 4; ++nt) wf[s][nt] = wfrag2<512, 4096>(L_W + wave * 16384 + (tr128 ^ (nt * 32)) + s * 8192);
#pragma unroll
      for (int s = 0; s < 2; ++s) asm volatile("" : "+v"(wf[s][0]), "+v"(wf[s][1]), "+v"(wf[s][2]), "+v"(wf[s][3]));
      asm volatile("s_waitcnt lgkmcnt(0)" ::: "memory");
#pragma unroll
      for (int i = 0; i < 4; ++i) dma16(wsl + (size_t)((wave & 3) * 32 + 8 * i) * DM + 512 + (wave >> 2) * 64 + (s128e ^ ((i & 1) * 32)), L_W + wave * 16384 + i * 1024);
    }
    float acc[8];
#pragma unroll
    for (int k = 0; k < 8; ++k) acc[k] = 0.f;
    u32 pw[16];
#pragma unroll
    for (int i4 = 0; i4 < 4; ++i4) {
      const u32x4 pv = *reinterpret_cast<const u32x4*>(pT + (wave * 4 + r) * 16 + i4 * 4);
#pragma unroll
      for (int j = 0; j < 4; ++j) pw[i4 * 4 + j] = pv[j];
    }
#define VREG(i) ((i) < NV0 ? vv[(i) < NV0 ? (i) : 0] : kv[(i) < NV0 ? 0 : (i) - NV0])
#define PV2(i) _Pragma("unroll") for (int k = 0; k < 4; ++k) { \
      acc[2 * k] = dot2(__builtin_amdgcn_perm(VREG((i) + 1)[k], VREG(i)[k], 0x05040100u), pw[(i) / 2], acc[2 * k]); \
      acc[2 * k + 1] = dot2(__builtin_amdgcn_perm(VREG((i) + 1)[k], VREG(i)[k], 0x07060302u), pw[(i) / 2], acc[2 * k + 1]); }
#define PV2I(i) { u32 pm[8]; \
      _Pragma("unroll") for (int k = 0; k < 4; ++k) { pm[2 * k] = __builtin_amdgcn_perm(VREG((i) + 1)[k], VREG(i)[k], 0x05040100u); pm[2 * k + 1] = __builtin_amdgcn_perm(VREG((i) + 1)[k], VREG(i)[k], 0x07060302u); } \
      asm volatile("" : "+v"(pm[0]), "+v"(pm[1]), "+v"(pm[2]), "+v"(pm[3]), "+v"(pm[4]), "+v"(pm[5]), "+v"(pm[6]), "+v"(pm[7])); \
      _Pragma("unroll") for (int k = 0; k < 8; ++k) acc[k] = dot2(pm[k], pw[(i) / 2], acc[k]); }
    u32x4 ml;
#pragma unroll
    for (int i = NV1; i < NL; i += 2) {
      kv[i - NV0] = LDV(i);
      asm volatile("" ::: "memory");
      kv[i + 1 - NV0] = LDV(i + 1);
      if (!RB && i == NL - 8) ml = ld_gran(rs, G_ML + (u ^ 128) * 64);
      asm volatile("" ::: "memory");
      if (!RB && i == NL - 2 && lane == 0) asm volatile("ds_add_u32 %0, %1" :: "v"(C_ISS), "v"(1u) : "memory");
      PV2(i - PVLAG)
#pragma unroll
      for (int k = 0; k < 8; ++k) asm volatile("" : "+v"(acc[k]));
    }
    WT3;
    LDS_WAIT(2, C_ML, 1);
    const float ew = st[16 + wave], Mb = st[40], Lb = st[41];
#define PSCALE(Mo, Lo) { const float eb_ = __expf(Mb - fmaxf(Mb, Mo)), eo_ = __expf((Mo) - fmaxf(Mb, Mo)), in_ = 256.f * frcp(fmaf(Lb, eb_, (Lo) * eo_)); psc = ew * eb_ * in_; osc = eo_ * in_; }
    u32x4 ox;
    const u32 o_x = bpair && (RB || wave == 0) ? G_OX + (u32)jB * 1024 + lane * 16 : OOB;
    s16x16 wx[2];
    u32 a_o, sidx, a_ow, o_0, o_1, o_p1;
    f32x4 d[5];
    if constexpr (!RB) {
      PAD(".Lc1");
      LDS_WAIT(2, C_ISS, 8);
#pragma unroll
      for (int s = 0; s < 2; ++s) wx[s] = wfrag2<512, 16384>(L_W + ((wave & 4) + 2 * s) * 16384 + (tr128 ^ ((wave & 3) * 32)));
      asm volatile("" : "+v"(wx[0]), "+v"(wx[1]));
      a_o = L_OB + (c & 7) * 272 + (c >> 3) * 128 + r * 16; sidx = c < 8 ? 0x4444 : 0xeeee;
      a_ow = L_OB + wave * 272 + (r & 1) * 128 + c * 8 + (r >> 1) * 4;
      o_0 = G_ACC + b_att * 5120 + (wave * 64 + lane) * 8; o_1 = lane < 16 ? G_ACC + b_att * 5120 + (512 + wave * 16 + lane) * 8 : OOB;
#pragma unroll
      for (int k = 0; k < 5; ++k) { d[k] = f32x4{0.f, 0.f, 0.f, 0.f}; asm volatile("" : "+v"(d[k])); }
      o_p1 = lane < R1 ? G_P1 + (lane * 4 + b_att) * 128 : OOB;
      asm volatile("" : "+v"(a_o), "+v"(a_ow), "+v"(sidx), "+v"(o_0), "+v"(o_1), "+v"(o_p1));
    }
#pragma unroll
    for (int i = NL - PVLAG; i < NL - 6; i += 2) {
      if (!RB && i == NL - 8) { ox = ld_gran(rs, o_x); asm volatile("" ::: "memory"); }
      if (!RB && i == NL - 10) PAD(".Lc0");
      PV2I(i)
#pragma unroll
      for (int k = 0; k < 8; ++k) asm volatile("" : "+v"(acc[k]));
    }
    float psc, osc;
    if constexpr (!RB) PSCALE(u2f(ml[0]), u2f(ml[2]))
    if constexpr (!RB) asm volatile("" : "+v"(psc), "+v"(osc));
    u32 otg = bpair && wave == 0 ? tg : 0u, tk = 1, mlb = 0, a_tk = C_FIN;
    u64 now;
    if constexpr (!RB) { mlb = (ml[1] ^ tg) | (ml[3] ^ tg); asm volatile("" : "+s"(otg), "+v"(mlb), "+v"(tk), "+v"(a_tk)); }
#pragma unroll
    for (int i = NL - 6; i < NL; i += 2) {
      if (!RB && i == NL - 2) {
        u32 bad;
        asm volatile("v_bitop3_b32 %0, %1, %2, %3 bitop3:0x7e\n\tv_or_b32 %0, %0, %4" : "=&v"(bad) : "v"(ox[1]), "v"(ox[3]), "s"(otg), "v"(mlb), "v"(VREG(NL - 1)[0]));
        if (__builtin_expect(__builtin_amdgcn_ballot_w64(bad != 0) != 0, 0)) {
          u32 of2[2] = {G_ML + (u32)(u ^ 128) * 64, o_x};
          u32x4 m2[2];
          gather<2>(rs, of2, m2, tg);
          PSCALE(u2f(m2[0][0]), u2f(m2[0][2]))
          ox = m2[1];
        }
        asm volatile("s_mov_b64 exec, 1\n\tds_add_rtn_u32 %0, %2, %0\n\ts_mov_b64 exec, -1\n\ts_memrealtime %1" : "+v"(tk), "=s"(now) : "v"(a_tk) : "memory");
      }
      PV2I(i)
#pragma unroll
      for (int k = 0; k < 8; ++k) asm volatile("" : "+v"(acc[k]));
    }
#undef PV2
#undef PV2I
#undef VREG
#pragma unroll
    for (int i = 0; i < 4; ++i) acc[i] = swap32_add(acc[i], acc[i + 4]);
#pragma unroll
    for (int i = 0; i < 2; ++i) acc[i] = swap16_add(acc[i], acc[i + 2]);
    if constexpr (!RB) {
      asm volatile("s_waitcnt lgkmcnt(0)" : "+v"(tk), "+s"(now), "+v"(acc[0]), "+v"(acc[1]) :: "memory");
      tk = __builtin_amdgcn_readfirstlane(tk);
#undef PSCALE
      *reinterpret_cast<u32*>(lds + a_ow) = cvt2(fmaf(acc[0], psc, u2f(ox[0]) * osc), fmaf(acc[1], psc, u2f(ox[2]) * osc));
      if (tk == 7) pace_post(rs, o_p1, (u32)now);
      STAMPT(6, 0); STAMPT(28, 448); WSTAMP(2); WF3;
      PAD(".Lc2");
      BAR();
      STAMPT(11, 0);
      u32x4 ao[2];
#pragma unroll
      for (int s = 0; s < 2; ++s) ao[s] = *reinterpret_cast<const u32x4*>(lds + a_o + s * 64);
      asm volatile("" ::: "memory");
#pragma unroll
      for (int s = 0; s < 2; ++s) {
#pragma unroll
        for (int k = 0; k < 4; ++k) d[k] = SMFMA(ao[s], wf[s][k], d[k], sidx);
        d[4] = SMFMA(ao[s], wx[s], d[4], sidx);
        __builtin_amdgcn_sched_barrier(0);
      }
      float tt[5];
#pragma unroll
      for (int k = 0; k < 5; ++k) { const f32x2 p = f32x2{d[k][0], d[k][1]} + f32x2{d[k][2], d[k][3]}; tt[k] = p[0] + p[1]; }
      float v0 = swap32_add(swap16_add(tt[0], tt[1]), swap16_add(tt[2], tt[3]));
      float v1 = cross_rows_sum(tt[4]);
      u64 q0 = fix40(v0), q1 = fix40(v1);
      asm volatile("" : "+v"(q0), "+v"(q1));
      if (__builtin_expect((__builtin_amdgcn_ballot_w64(!(fabsf(v0) < 8192.f)) | __builtin_amdgcn_ballot_w64(!(fabsf(v1) < 8192.f))) != 0, 0)) {
        float f0 = v0 - 8192.f * rintf(v0 * 0x1p-13f), f1 = v1 - 8192.f * rintf(v1 * 0x1p-13f);
        f0 = fabsf(f0) <= 4096.f ? f0 : 0.f; f1 = fabsf(f1) <= 4096.f ? f1 : 0.f;
        add_f32(a.ws + G_ACF + ((o_0 - G_ACC) >> 1), (v0 - f0) * 0x1p-8f);
        and_u64(a.ws + o_0, ~0ull >> 1);
        if (lane < 16) { add_f32(a.ws + G_ACF + ((o_1 - G_ACC) >> 1), (v1 - f1) * 0x1p-8f); and_u64(a.ws + o_1, ~0ull >> 1); }
        VM0();
        q0 = fix40(f0); q1 = fix40(f1);
      }
      asm volatile("buffer_atomic_add_x2 %0, %1, %4, 0 offen\n\tbuffer_atomic_add_x2 %2, %3, %4, 0 offen" :: "v"(q0), "v"(o_0), "v"(q1), "v"(o_1), "s"(rs) : "memory");
      __builtin_amdgcn_sched_barrier(0);
      STAMPT(13, 0);
    }
  if constexpr (RB) {
    *reinterpret_cast<f32x2*>(lds + L_RED + wave * 512 + lane * 8) = f32x2{acc[0] * ew, acc[1] * ew};
    if (lds_ticket(C_ISS, lane) == 7) {
      f32x2 sm[8];
#pragma unroll
      for (int k = 0; k < 8; ++k) sm[k] = *reinterpret_cast<const f32x2*>(lds + L_RED + k * 512 + lane * 8);
      const f32x2 sx = ((sm[0] + sm[1]) + (sm[2] + sm[3])) + ((sm[4] + sm[5]) + (sm[6] + sm[7]));
      st_gran(rs, o_x, f2u(sx[0]), f2u(sx[1]), tg);
    }
    STAMPT(6, 0); STAMPT(28, 448); WSTAMP(2); WF3;
#define GM_OK(ok) bool ok = true; _Pragma("unroll") for (int i = 0; i < 9; ++i) ok &= (i % 3 == 2 && lane >= 32) | (gm[i][1] == tg)
    GM_OK(ok0);
    if (!__all(ok0)) {
#pragma nounroll
      for (unsigned spins = 0; spins < SPIN_CAP; ++spins) {
        LD_GM;
        GM_OK(ok);
        if (__all(ok)) break;
        __builtin_amdgcn_s_sleep(1);
      }
    }
    f32x2 xf[3], ga[3], xc[3], gc[3];
    u32x4 aw[3];
#pragma unroll
    for (int s = 0; s < 3; ++s) {
      const bool on = s < 2 || lane < 32;
      const u32 cw = on ? cvt2(1.f + lo16(gm[6 + s][0]), 1.f + hi16(gm[6 + s][0])) : 0u, sw = gm[3 + s][0], xv = on ? xw[s] : 0u;
      xf[s] = f32x2{lo16(xv), hi16(xv)};
      ga[s] = f32x2{lo16(gm[s][0]), hi16(gm[s][0])};
      xc[s] = xf[s] * f32x2{lo16(cw), hi16(cw)};
      gc[s] = ga[s] * f32x2{lo16(cw), hi16(cw)};
      const u32x4 ac = QUAD(cw), as = QUAD(sw);
      aw[s] = (c & 1) ? as : ac;
    }
    f32x2 b1f = {lo16(b1w), hi16(b1w)};
#pragma unroll
    for (int i = 0; i < 3; ++i) asm volatile("" : "+v"(xf[i]), "+v"(ga[i]), "+v"(xc[i]), "+v"(gc[i]), "+v"(aw[i]));
    u32 o_a = G_ACC + (c >> 2) * 5120 + wave * 640 + r * 64 + (c & 3) * 16;
    u32 o_a2 = lane < 32 ? o_a + 512 : OOB, m_a2 = lane < 32 ? 0u : ~0u;
    u32 a_red = L_RED + (wave >> 1) * 1024 + (c & 1) * 512 + (r >> 1) * 256 + (c >> 2) * 64 + (r & 1) * 32 + (c & 2) * 8 + (wave & 1) * 8;
    u32 a_fin = L_RED + lane * 16;
    u32 a_tc = L_XA + 1024 + lane * 16;
    u32 a_out = G_OUT + ((wave * 4 + r) * 56 + jB) * 256 + c * 16;
    u32 a_st = L_XA + 64 * (c >> 2), a_sw = (lane & 50) == 0 ? L_XA + (c >> 2) * 64 + wave * 8 + (c & 1) * 4 : L_OB + lane * 4;
    u32 a_w1 = L_W + wave * 16384 + 4096 + (8 * r + (c >> 2)) * 64 + (r & 1) * 32 + (c & 3) * 8;
    u32 a_w1b = a_w1 + 4096 - (r >> 1) * 1024;
    u32 a_tw = L_XA + 1024 + wave * 1024 + (c & 2) * 256 + (r >> 1) * 256 + (c >> 2) * 64 + (r & 1) * 32 + (c & 1) * 8;
    u32 o_l = lane < 4 ? G_P1 + ((jB % R1) * 4 + lane) * 128 : OOB, n_l = lane < 4 ? ((lane & 1) ? 52u : 49u) : 0u;
    u32 o_n = lane < 4 && jB < 40 && wave == 5 ? G_NT + (jB * 4 + lane) * 128 : OOB;
    const int xs = (jB % 5) >> 1;
    u32 o_x1 = wave == jB / 5 && (r >> 1) == (jB % 5 & 1) ? G_OUT + (((jB >> 2) * 4 + 2 * (r & 1) + ((c >> 1) & 1)) * 56 + 54) * 256 + (c >> 2) * 64 + (jB & 3) * 16 + (c & 1) * 8 : jB == 53 && t == 0 ? WS_EPOCH - 4 : OOB;
    u32 o_p2 = lane < R2 ? G_P2 + (lane * 4 + jB % 4) * 128 : OOB, tk2 = 1, one = 1, a_tk2 = C_WO;
    u32 qm[4];
#pragma unroll
    for (int k = 0; k < 4; ++k) { qm[k] = (c & 3) == k ? ~0u : 0u; asm volatile("" : "+v"(qm[k])); }
    float kof = -40960.f;
    asm volatile("" : "+v"(o_p2), "+v"(tk2), "+v"(one), "+v"(a_tk2), "+v"(kof));
    float eps2 = (c & 1) ? 1e-5f / 8 : 0.f;
    asm volatile("" : "+v"(o_a), "+v"(o_a2), "+v"(m_a2), "+v"(a_red), "+v"(a_fin), "+v"(a_out), "+v"(a_st), "+v"(a_sw), "+v"(a_tc), "+v"(a_w1), "+v"(a_w1b), "+v"(a_tw), "+v"(o_l), "+v"(n_l), "+v"(o_n), "+v"(o_x1), "+v"(eps2), "+v"(b1f));
    u32 a_wg[4];
    u32x4 og[3];
#pragma unroll
    for (int k = 0; k < 4; ++k) {
      a_wg[k] = L_W + 9216 + wave * 1024 + r * (32768 - 1024) + (tr128 ^ (k * 32));
      asm volatile("" : "+v"(a_wg[k]));
    }
    og[0] = u32x4{tg, tg, tg, tg}; og[1] = og[0]; og[2] = og[0];
    u32 o_3 = (4 + 4 * wave) * (4 * 56 * 256), a_3 = 4096 + wave * (16384 - 1024);
    asm volatile("" : "+v"(og[0]), "+v"(og[1]), "+v"(og[2]), "+s"(o_3), "+s"(a_3));
    u32x2 ox1 = {tg, tg};
    asm volatile("" : "+v"(wp1), "+v"(wp2), "+v"(ox1));
    STAMPT(2, 0);
    u64 kp1[3] = {0, 0, 0};
    if (lds_ticket(C_FIN, lane) == 0) {
      u32 clk;
      pace_wait<8>(rs, lane == 3 ? o_l : OOB, lane == 3 ? (u32)TD_ : 0u, kp1, clk);
      lds_arrive(C_TK, lane);
    } else {
      LDS_WAIT(2, C_TK, 1);
    }
    PAD(".Lb1");
    STAMPT(22, 0);
#pragma unroll
    for (int k = 0; k < 5; ++k) dma16(wp1 + (size_t)(16 * k) * FF, L_W + wave * 16384 + 4096 + k * 1024);
    asm volatile("s_barrier" ::: "memory");
#pragma unroll
    for (int k = 0; k < 5; ++k) dma16(wp2 + k * 64, L_W + wave * 16384 + 9216 + k * 1024);
    STAMPT(8, 0);
    PAD(".Lb2");
    asm volatile("s_waitcnt vmcnt(5)" ::: "memory");
    STAMPT(12, 0);
    s16x8 wf[2][3], wg[12];
#pragma unroll
    for (int s = 0; s < 3; ++s)
#pragma unroll
      for (int nt = 0; nt < 2; ++nt) wf[nt][s] = wfrag<256>(((s == 2 ? a_w1b : a_w1) ^ (nt * 32)) + (s & 1) * 2048);
    asm volatile("" : "+v"(wf[0][0]), "+v"(wf[0][1]), "+v"(wf[0][2]), "+v"(wf[1][0]), "+v"(wf[1][1]), "+v"(wf[1][2]));
    {
      f32x4 acc[2] = {{0.f, 0.f, 0.f, 0.f}, {0.f, 0.f, 0.f, 0.f}};
#pragma unroll
      for (int s = 0; s < 3; ++s)
#pragma unroll
        for (int nt = 0; nt < 2; ++nt) acc[nt] = MFMA(wf[nt][s], aw[s], acc[nt]);
      const f32x4 v = (c & 2) ? acc[1] : acc[0];
      *reinterpret_cast<f32x2*>(lds + a_tw) = f32x2{v[0], v[1]};
      *reinterpret_cast<f32x2*>(lds + a_tw + 16) = f32x2{v[2], v[3]};
    }
    u32x4 pa[3];
#define SWEEP pa[0] = ld_gran(rs, o_a); pa[1] = ld_gran(rs, o_a + 256); pa[2] = ld_gran(rs, o_a2); asm volatile("" ::: "memory")
    PACE_KEEP(kp1);
    VM0();
    __builtin_amdgcn_raw_buffer_store_b32(ox1[1], rs, o_n, 0, 16);
    STAMPT(21, 0);
    if (wave == 0) {
      u32 clk;
      pace_wait<5>(rs, o_l, n_l, kp1, clk);
      STAMPT(9, 0);
      pace_due(clk, D1_);
    }
    BAR();
    SWEEP;
    STAMPT(18, 0);
    f32x2 Tc, Ts;
    asm volatile("" : "=v"(Tc), "=v"(Ts));
#pragma unroll
    for (int k = 0; k < 12; ++k) asm volatile("" : "=v"(wg[k]));
    if (wave < 4) {
      f32x4 tp[8];
#pragma unroll
      for (int k = 0; k < 8; ++k) tp[k] = *reinterpret_cast<const f32x4*>(lds + a_tc + k * 1024);
#pragma unroll
      for (int k = 0; k < 8; ++k) wg[k] = wfrag<512>(a_wg[k & 3] + (k >> 2) * 16384);
      if (wave < 2) {
#pragma unroll
        for (int k = 0; k < 4; ++k) wg[8 + k] = wfrag<512>(a_wg[k] + a_3);
      }
      const f32x4 ts = ((tp[0] + tp[1]) + (tp[2] + tp[3])) + ((tp[4] + tp[5]) + (tp[6] + tp[7]));
      Tc = f32x2{ts[0], ts[1]};
      Ts = f32x2{ts[2], ts[3]} + b1f;
    }
    asm volatile("" : "+v"(Tc), "+v"(Ts), "+v"(wg[0]), "+v"(wg[1]), "+v"(wg[2]), "+v"(wg[3]), "+v"(wg[4]), "+v"(wg[5]), "+v"(wg[6]), "+v"(wg[7]), "+v"(wg[8]), "+v"(wg[9]), "+v"(wg[10]), "+v"(wg[11]));
    STAMPT(5, 0);
    PAD(".Lb3");
    asm volatile("" : "+v"(pa[0]), "+v"(pa[1]), "+v"(pa[2]));
#define HS (pa[0][1] & pa[0][3] & pa[1][1] & pa[1][3] & ((pa[2][1] & pa[2][3]) | m_a2))
    if (__builtin_expect(__builtin_amdgcn_ballot_w64(HS < 0x90000000u) != 0, 0)) {
#pragma nounroll
      for (unsigned spins = 0; spins < SPIN_CAP; ++spins) {
        SWEEP;
        if (__builtin_amdgcn_ballot_w64((HS & (1u << 28)) == 0) == 0) break;
      }
      if (__builtin_amdgcn_ballot_w64(HS < 0x90000000u) != 0) {
#pragma unroll
        for (int i = 0; i < 3; ++i) {
          const u32x2 cf = __builtin_amdgcn_raw_buffer_load_b64(rs, i < 2 || lane < 32 ? G_ACF + ((o_a - G_ACC) >> 1) + i * 128 : OOB, 0, 16);
          const f32x2 cv = {u2f(cf[0]), u2f(cf[1])};
          xf[i] += ga[i] * cv; xc[i] += gc[i] * cv;
          pa[i][1] |= 1u << 31; pa[i][3] |= 1u << 31;
        }
      }
    }
    PACE_KEEP(kp1);
    u64 now2;
    asm volatile("s_mov_b64 exec, 1\n\tds_add_rtn_u32 %0, %5, %0\n\ts_mov_b64 exec, -1\n\ts_memrealtime %1" : "+v"(tk2), "=s"(now2), "+v"(pa[0]), "+v"(pa[1]), "+v"(pa[2]) : "v"(a_tk2) : "memory");
    STAMPT(14, 0); STAMPT(1, 448); STAMPT(4, 256); STAMPT(7, 192);
    f32x2 x1[3];
    {
      f32x2 at[3];
      u32 w[3];
#pragma unroll
      for (int s = 0; s < 3; ++s) {
        f32x2 hi;
        asm("v_fma_f32 %0, %2, %4, %5\n\tv_fma_f32 %1, %3, %4, %5" : "=&v"(hi[0]), "=&v"(hi[1]) : "v"(pa[s][1]), "v"(pa[s][3]), "s"(-0x1p110f), "v"(kof));
        at[s] = __builtin_elementwise_fma(f32x2{(float)pa[s][0], (float)pa[s][2]}, f32x2{0x1p-40f, 0x1p-40f}, hi);
      }
      __builtin_amdgcn_sched_barrier(0);
      f32x2 v[3];
#pragma unroll
      for (int s = 0; s < 3; ++s) v[s] = __builtin_elementwise_fma(gc[s], at[s], xc[s]);
      __builtin_amdgcn_sched_barrier(0);
#pragma unroll
      for (int s = 0; s < 3; ++s) w[s] = cvt2(v[s][0], v[s][1]);
      u32x4 av[3];
      __builtin_amdgcn_sched_barrier(0);
      f32x4 acc[2] = {{0.f, 0.f, 0.f, 0.f}, {0.f, 0.f, 0.f, 0.f}};
#pragma unroll
      for (int s = 0; s < 3; ++s) av[s] = QUAD(w[s]);
      __builtin_amdgcn_sched_barrier(0);
      acc[0] = MFMA(wf[0][0], av[0], acc[0]);
      x1[0] = __builtin_elementwise_fma(ga[0], at[0], xf[0]);
      x1[1] = __builtin_elementwise_fma(ga[1], at[1], xf[1]);
      __builtin_amdgcn_sched_barrier(0);
      acc[1] = MFMA(wf[1][0], av[0], acc[1]);
      x1[2] = __builtin_elementwise_fma(ga[2], at[2], xf[2]);
      f32x2 q2 = x1[0] * x1[0];
      __builtin_amdgcn_sched_barrier(0);
      {
        u32 t0, t1;
        asm volatile("s_waitcnt lgkmcnt(0)\n\tv_readfirstlane_b32 %0, %2\n\tv_mov_b32 %1, %3\n\ts_cmp_eq_u32 %0, 7\n\ts_cselect_b64 exec, -1, 0\n\t"
                     "buffer_atomic_umax %1, %4, %5, 0 offen offset:4\n\tbuffer_atomic_add %6, %4, %5, 0 offen\n\ts_mov_b64 exec, -1"
                     : "=&s"(t0), "=&v"(t1) : "v"(tk2), "s"((u32)now2), "v"(o_p2), "s"(rs), "v"(one), "s"(now2) : "scc");
      }
      __builtin_amdgcn_sched_barrier(0);
      acc[0] = MFMA(wf[0][1], av[1], acc[0]);
      f32x2 s2 = x1[0] + x1[1];
      q2 = __builtin_elementwise_fma(x1[1], x1[1], q2);
      __builtin_amdgcn_sched_barrier(0);
      acc[1] = MFMA(wf[1][1], av[1], acc[1]);
      s2 += x1[2];
      q2 = __builtin_elementwise_fma(x1[2], x1[2], q2);
      __builtin_amdgcn_sched_barrier(0);
      acc[0] = MFMA(wf[0][2], av[2], acc[0]);
      const float ps = s2[0] + s2[1], pq = q2[0] + q2[1];
      __builtin_amdgcn_sched_barrier(0);
      acc[1] = MFMA(wf[1][2], av[2], acc[1]);
      const float t1 = ps + dpp<XOR1>(ps), t2 = pq + dpp<XOR1>(pq);
      float z = (c & 1) ? t2 : t1;
      z += dpp<XOR2>(z);
      z = cross_rows_sum(z);
      const f32x4 vs = (c & 1) ? acc[1] : acc[0];
      *reinterpret_cast<float*>(lds + a_sw) = fmaf(z, 1.f / DM, eps2);
      *reinterpret_cast<f32x2*>(lds + a_red) = (c & 2) ? f32x2{vs[2], vs[3]} : f32x2{vs[0], vs[1]};
    }
    asm volatile("" ::: "memory");
    __builtin_amdgcn_sched_barrier(0);
    const f32x2 xo = xs == 0 ? x1[0] : xs == 1 ? x1[1] : x1[2];
    ox1[0] = cvt2(xo[0], xo[1]);
    __builtin_amdgcn_raw_buffer_store_b64(ox1, rs, o_x1, 0, 16);
    asm volatile("" ::: "memory");
    __builtin_amdgcn_sched_barrier(0);
    PAD(".Lb4");
    BAR();
    STAMPT(19, 0);
    if (wave < 4) {
      f32x4 sv[4];
      f32x4 sp[4];
#pragma unroll
      for (int k = 0; k < 4; ++k) sv[k] = *reinterpret_cast<const f32x4*>(lds + a_st + k * 16);
#pragma unroll
      for (int k = 0; k < 4; ++k) sp[k] = *reinterpret_cast<const f32x4*>(lds + a_fin + k * 1024);
      const f32x4 s4 = (sv[0] + sv[1]) + (sv[2] + sv[3]);
      const f32x4 sq = (sp[0] + sp[1]) + (sp[2] + sp[3]);
      const f32x2 s = f32x2{sq[0], sq[1]} + f32x2{sq[2], sq[3]};
      const float mean = s4[0] + s4[2];
      const float rstd = __builtin_amdgcn_rsqf(fmaxf(fmaf(-mean, mean, s4[1] + s4[3]), 1e-5f));
      float f[2];
#pragma unroll
      for (int e = 0; e < 2; ++e) {
        const float hh = fmaf(rstd, fmaf(-mean, Tc[e], s[e]), Ts[e]);
        const float e2 = __builtin_amdgcn_exp2f(hh * fmaf(hh * hh, 0.1029432397f, 2.302208198f));
        f[e] = fmaf(-hh, frcp(e2 + 1.f), hh);
      }
      const u32 w = cvt2(f[0], f[1]);
      u32x4 av[4];
      f32x4 acc[3] = {{0.f, 0.f, 0.f, 0.f}, {0.f, 0.f, 0.f, 0.f}, {0.f, 0.f, 0.f, 0.f}};
      asm volatile("s_nop 1" :: "v"(w));
#define AVK(k) asm volatile("v_and_b32_dpp %0, %4, %5 quad_perm:[0,0,0,0] row_mask:0xf bank_mask:0xf\n\tv_and_b32_dpp %1, %4, %5 quad_perm:[1,1,1,1] row_mask:0xf bank_mask:0xf\n\t" \
        "v_and_b32_dpp %2, %4, %5 quad_perm:[2,2,2,2] row_mask:0xf bank_mask:0xf\n\tv_and_b32_dpp %3, %4, %5 quad_perm:[3,3,3,3] row_mask:0xf bank_mask:0xf" \
        : "=&v"(av[k][0]), "=&v"(av[k][1]), "=&v"(av[k][2]), "=&v"(av[k][3]) : "v"(w), "v"(qm[k]))
      AVK(0); AVK(1);
      __builtin_amdgcn_sched_barrier(0);
      acc[0] = MFMA(wg[0], av[0], acc[0]);
      __builtin_amdgcn_sched_barrier(0);
      AVK(2);
      __builtin_amdgcn_sched_barrier(0);
      acc[0] = MFMA(wg[1], av[1], acc[0]);
      __builtin_amdgcn_sched_barrier(0);
      AVK(3);
      __builtin_amdgcn_sched_barrier(0);
      acc[0] = MFMA(wg[2], av[2], acc[0]);
      acc[0] = MFMA(wg[3], av[3], acc[0]);
      acc[1] = MFMA(wg[4], av[0], acc[1]);
      acc[1] = MFMA(wg[5], av[1], acc[1]);
      __builtin_amdgcn_sched_barrier(0);
      og[0][0] = cvt2(acc[0][0], acc[0][1]); og[0][2] = cvt2(acc[0][2], acc[0][3]);
      __builtin_amdgcn_sched_barrier(0);
      acc[1] = MFMA(wg[6], av[2], acc[1]);
      __builtin_amdgcn_raw_buffer_store_b128(og[0], rs, a_out, 0, 16);
      __builtin_amdgcn_sched_barrier(0);
      acc[1] = MFMA(wg[7], av[3], acc[1]);
      if (wave < 2) {
        acc[2] = MFMA(wg[8], av[0], acc[2]);
        acc[2] = MFMA(wg[9], av[1], acc[2]);
        __builtin_amdgcn_sched_barrier(0);
        asm volatile("" : "+v"(acc[1]));
        og[1][0] = cvt2(acc[1][0], acc[1][1]); og[1][2] = cvt2(acc[1][2], acc[1][3]);
        __builtin_amdgcn_sched_barrier(0);
        acc[2] = MFMA(wg[10], av[2], acc[2]);
        __builtin_amdgcn_raw_buffer_store_b128(og[1], rs, a_out, 20 * 56 * 256, 16);
        __builtin_amdgcn_sched_barrier(0);
        acc[2] = MFMA(wg[11], av[3], acc[2]);
        og[2][0] = cvt2(acc[2][0], acc[2][1]); og[2][2] = cvt2(acc[2][2], acc[2][3]);
        __builtin_amdgcn_raw_buffer_store_b128(og[2], rs, a_out, o_3, 16);
      } else {
        og[1][0] = cvt2(acc[1][0], acc[1][1]); og[1][2] = cvt2(acc[1][2], acc[1][3]);
        __builtin_amdgcn_raw_buffer_store_b128(og[1], rs, a_out, 20 * 56 * 256, 16);
      }
    }
    STAMPT(15, 0); STAMPT(3, 448); STAMPT(20, 256);
    asm volatile(".Lpf1:");
  } else if ((!half || (g >> 5) == 1) && wave == CW_) {
    const int cm = (half ? 4 : g >> 5) * 4 + (g & 3);
    const int cg = (cm >> 1) * 16 + ((g >> 3) & 3) * 4 + (cm & 1) * 2 + ((g >> 2) & 1), b = cg & 3, q = lane & 3, jj = lane >> 2;
    const int c4 = (cg >> 4) * 16 + q * 4 + ((cg >> 2) & 3), col = c4 * 4 + r;
    const float b2v = bf2f(a.b2[col]);
    float gbv;
    const __amdgpu_buffer_rsrc_t ro = __builtin_amdgcn_make_buffer_rsrc((void*)a.out, 0, NB * DM * 2, 0x00020000);
    u32 oo = lane < 4 ? (b * DM + col) * 2 : OOB;
    asm volatile("" : "+v"(oo));
    asm volatile("" ::: "memory");
    {
      u32 offs[1], va[1], vb[1];
      offs[0] = lane < 4 ? G_MF + 30720 + (c4 >> 1) * 128 + b * 32 + (c4 & 1) * 16 : OOB;
      gather_m<1>(rs, offs, va, vb, tg);
      PAD(".Lc3");
      const u32 gbw0 = __builtin_amdgcn_ds_bpermute(q * 4, va[0]), gbw1 = __builtin_amdgcn_ds_bpermute(q * 4, vb[0]);
      const u32 gbw = (r & 2) ? gbw1 : gbw0;
      gbv = (r & 1) ? hi16(gbw) : lo16(gbw);
      const u32 o_l = lane == 0 ? G_NT + cg * 128 : OOB;
#pragma nounroll
      for (unsigned spins = 0; spins < SPIN_CAP; ++spins) {
        if (__builtin_amdgcn_readfirstlane(__builtin_amdgcn_raw_buffer_load_b32(rs, o_l, 0, 16)) == tg) break;
        __builtin_amdgcn_s_sleep(1);
      }
    }
    u32 offs[5];
#pragma unroll
    for (int i = 0; i < 4; ++i) offs[i] = (jj + 16 * i < 54) ? G_OUT + ((cg >> 2) * 56 + jj + 16 * i) * 256 + b * 64 + q * 16 : OOB;
    offs[4] = c < 4 ? G_OUT + ((cg >> 2) * 56 + 54) * 256 + b * 64 + q * 16 : OOB;
    u32 tg3 = jj < 6 ? tg : 0u, tg4 = c < 4 ? tg : 0u, xsel = (r & 1) ? 0x03020c0cu : 0x01000c0cu;
    float gb2 = gbv * b2v;
    if (r & 2) xsel += 0x04040000u;
    asm volatile("" : "+v"(gbv), "+v"(gb2), "+v"(tg3), "+v"(tg4), "+v"(xsel), "+v"(offs[0]), "+v"(offs[1]), "+v"(offs[2]), "+v"(offs[3]), "+v"(offs[4]));
    u64 kp2[3];
    {
      u32 clk;
      pace_wait<5>(rs, lane < 4 ? G_P2 + ((cg % R2) * 4 + lane) * 128 : OOB, lane < 4 ? (57u - lane) / 4 : 0u, kp2, clk);
      STAMPT(23, CW_ * 64);
      pace_due(clk, D2_);
      STAMPT(24, CW_ * 64);
    }
    u32x4 gv[5];
#pragma unroll
    for (int i = 0; i < 5; ++i) gv[i] = ld_gran(rs, offs[i]);
    asm volatile("" ::: "memory");
    PAD(".Lc4");
    {
      u32 bad, t1, t2;
      asm("v_bitop3_b32 %0, %3, %4, %13 bitop3:0x7e\n\tv_bitop3_b32 %1, %5, %6, %13 bitop3:0x7e\n\tv_bitop3_b32 %2, %7, %8, %13 bitop3:0x7e\n\tv_or3_b32 %0, %0, %1, %2\n\t"
          "v_bitop3_b32 %1, %9, %10, %14 bitop3:0x7e\n\tv_bitop3_b32 %2, %11, %12, %15 bitop3:0x7e\n\tv_or3_b32 %0, %0, %1, %2"
          : "=&v"(bad), "=&v"(t1), "=&v"(t2) : "v"(gv[0][1]), "v"(gv[0][3]), "v"(gv[1][1]), "v"(gv[1][3]), "v"(gv[2][1]), "v"(gv[2][3]), "v"(gv[3][1]), "v"(gv[3][3]),
            "v"(gv[4][1]), "v"(gv[4][3]), "s"(tg), "v"(tg3), "v"(tg4));
      if (__builtin_expect(__builtin_amdgcn_ballot_w64(bad != 0) != 0, 0)) {
        PAD(".Lc5");
#pragma nounroll
        for (unsigned spins = 0; spins < SPIN_CAP; ++spins) {
          bool ok = true;
#pragma unroll
          for (int i = 0; i < 5; ++i) { offs[i] = (gv[i][1] == tg) & (gv[i][3] == tg) ? OOB : offs[i]; ok &= offs[i] == OOB; }
          if (__all(ok)) break;
          u32x4 g2[5];
#pragma unroll
          for (int i = 0; i < 5; ++i) g2[i] = ld_gran(rs, offs[i]);
#pragma unroll
          for (int i = 0; i < 5; ++i) gv[i] = offs[i] == OOB ? gv[i] : g2[i];
          asm volatile("" ::: "memory");
        }
      }
    }
    STAMPT(16, CW_ * 64);
    float s0 = lo16(gv[0][0]), s1 = hi16(gv[0][0]), s2 = lo16(gv[0][2]), s3 = hi16(gv[0][2]);
#pragma unroll
    for (int i = 1; i < 4; ++i) { s0 = addlo(gv[i][0], s0); s1 = addhi(gv[i][0], s1); s2 = addlo(gv[i][2], s2); s3 = addhi(gv[i][2], s3); }
    const float base = u2f(__builtin_amdgcn_perm(gv[4][2], gv[4][0], xsel)) + gb2;
    float s = swap16_add(swap32_add(s0, s2), swap32_add(s1, s3));
    s += dpp<ROR4>(s); s += dpp<XOR8>(s);
    {
      const u32 ov = f2u(fmaf(gbv, s, base));
      const auto p1 = __builtin_amdgcn_permlane16_swap(ov, ov, false, false);
      const u32 w2 = cvt2(u2f(p1[0]), u2f(p1[1]));
      const auto p2 = __builtin_amdgcn_permlane32_swap(w2, w2, false, false);
      __builtin_amdgcn_raw_buffer_store_b64(u32x2{p2[0], p2[1]}, ro, oo, 0, 0);
    }
    STAMPT(17, CW_ * 64);
    PACE_KEEP(kp2);
  }
  };
  if (bpair && ahalf) body(std::true_type{}); else body(std::false_type{});
  STAMP_FLUSH;
}

extern "C" void kernel_launch(void* const* d_in, const int* in_sizes, int n_in, void* d_out, int out_size, void* d_ws,
                              size_t ws_size, hipStream_t stream) {
  Args a;
  a.x = (const bf16*)d_in[0];
  a.kc = (const bf16*)d_in[1];
  a.vc = (const bf16*)d_in[2];
  a.wq = (const bf16*)d_in[3];
  a.wo = (const bf16*)d_in[4];
  a.w1 = (const bf16*)d_in[6];
  a.b1 = (const bf16*)d_in[7];
  a.w2 = (const bf16*)d_in[8];
  a.b2 = (const bf16*)d_in[9];
  a.c = (const bf16*)d_in[10];
  a.wmod = (const bf16*)d_in[11];
  a.out = (bf16*)d_out;
  a.ws = (unsigned char*)d_ws;
  mega_kernel<<<NWG, NT, 0, stream>>>(a);
}
```
